```python
import torch

def _softmax(x, axis=-1):
    return torch.softmax(x, dim=axis)
def _gelu(x):
    return torch.nn.functional.gelu(x, approximate="tanh")
def _silu(x):
    return torch.nn.functional.silu(x)
def _sigmoid(x):
    return torch.sigmoid(x)
def _layernorm(x, axis=-1, eps=1e-5):
    m = x.mean(dim=axis, keepdim=True)
    v = x.var(dim=axis, keepdim=True, unbiased=False)
    return (x - m) / torch.sqrt(v + eps)
def _scatter_add(tgt, idx, src, axis=0):
    return tgt.clone().index_add_(axis, idx, src)
def _segment_sum(src, seg, n):
    out = torch.zeros((n,) + src.shape[1:], dtype=src.dtype)
    return out.index_add_(0, seg, src)
def _pad(x, pw):
    flat = tuple(v for pair in reversed(pw) for v in pair)
    return torch.nn.functional.pad(x, flat)

kernel_name = 'standalone__channel_attn_cbam__vision'

def setup_inputs():

    _w = (lambda a: (lambda t: t.to(torch.bfloat16) if t.is_floating_point() else t)(torch.as_tensor(a)))
    H = W = 256
    C = 384
    r = max(C // 4, 1)
    return {
        "x": _w((lambda *s: torch.randn(*s, dtype=torch.bfloat16) * 0.1)(1, H, W, C) + (lambda *s: torch.randn(*s, dtype=torch.bfloat16) * 0.1)(1, 1, 1, C) * 2.0),
        "w1": _w((lambda *s: torch.randn(*s, dtype=torch.bfloat16) * 0.1)(C, r)),
        "w2": _w((lambda *s: torch.randn(*s, dtype=torch.bfloat16) * 0.1)(r, C)),
    }

def reference(x, w1, w2):
    B, H, W, C = x.shape
    xf = x.reshape(B, H * W, C)
    gap = torch.mean(xf, 1)
    gmp = torch.amax(xf, 1)
    h_a = gap @ w1
    h_a = h_a * (lambda x, dt: x.to(dt))(h_a > 0, h_a.dtype)
    h_m = gmp @ w1
    h_m = h_m * (lambda x, dt: x.to(dt))(h_m > 0, h_m.dtype)
    scale = _sigmoid(h_a @ w2 + h_m @ w2)
    return x * scale[:, None, None, :]
```

```cpp
#include <hip/hip_runtime.h>
#include <hip/hip_bf16.h>
#include <stdint.h>

constexpr int LAUX = 19;
constexpr int SAUX = 18;
constexpr int HAUX = 16;
constexpr unsigned PACE = 40;
constexpr unsigned PACE_RED = 20;
constexpr int NREP = 4;

typedef unsigned v4u __attribute__((ext_vector_type(4)));
typedef unsigned v2u __attribute__((ext_vector_type(2)));
typedef float v4f __attribute__((ext_vector_type(4)));
typedef float v2f __attribute__((ext_vector_type(2)));
typedef short v4s __attribute__((ext_vector_type(4)));
typedef __bf16 v2bf __attribute__((ext_vector_type(2)));
typedef __bf16 v8bf __attribute__((ext_vector_type(8)));
typedef __bf16 v16bf __attribute__((ext_vector_type(16)));
typedef unsigned v8u __attribute__((ext_vector_type(8)));
typedef unsigned long long u64;
#define LAS __attribute__((address_space(3)))

constexpr int HW = 65536, C = 384, R = 96;
constexpr int NWG = 256, NT = 768, NL = 16;
constexpr unsigned XBYTES = (unsigned)HW * C * 2;
constexpr int NRED = 24;
constexpr unsigned WS_E = 0;
constexpr unsigned WS_P = 4096;
constexpr unsigned WS_F = WS_P + NRED * NWG * 128;
constexpr unsigned WS_END = WS_F + NREP * 3072;
constexpr unsigned L_W1 = 0;
constexpr unsigned L_W2 = L_W1 + C * R * 2;
constexpr unsigned L_PART = L_W2 + C * R * 2;
constexpr unsigned L_IN = L_PART;
constexpr unsigned L_H = L_IN + 1536;
constexpr unsigned L_SC = L_H + 512;
constexpr unsigned L_RED = L_PART + 4 * 192 * 16;
constexpr unsigned L_CNT = L_RED + 1024;
constexpr unsigned L_DUMMY = L_CNT + 16;
constexpr unsigned L_END = L_DUMMY + 256;
constexpr unsigned LDS_BYTES = L_END;
static_assert(LDS_BYTES <= 160 * 1024 && LDS_BYTES > 80 * 1024 && L_SC + 1536 <= L_RED, "lds");
constexpr unsigned SPIN_CAP = 1u << 20;

__device__ __forceinline__ float f_lo(unsigned d) { return __uint_as_float(d << 16); }
__device__ __forceinline__ float f_hi(unsigned d) { return __uint_as_float(d & 0xffff0000u); }
__device__ __forceinline__ float vmax(float a, float b) { return __builtin_elementwise_maximum(a, b); }
__device__ __forceinline__ float vmax3(float a, float b, float c) { return vmax(vmax(a, b), c); }
__device__ __forceinline__ unsigned pk_bf16(float a, float b) { v2bf r = {(__bf16)a, (__bf16)b}; return __builtin_bit_cast(unsigned, r); }
__device__ __forceinline__ float rbf(float a) { return __uint_as_float(pk_bf16(a, a) << 16); }
template <int CTRL> __device__ __forceinline__ float dpp(float x) {
    return __builtin_bit_cast(float, __builtin_amdgcn_mov_dpp(__builtin_bit_cast(int, x), CTRL, 0xf, 0xf, true));
}
__device__ __forceinline__ void swap32(float& a, float& b) {
    auto r = __builtin_amdgcn_permlane32_swap(__float_as_uint(a), __float_as_uint(b), false, false);
    a = __uint_as_float(r[0]); b = __uint_as_float(r[1]);
}
__device__ __forceinline__ void swap16(float& a, float& b) {
    auto r = __builtin_amdgcn_permlane16_swap(__float_as_uint(a), __float_as_uint(b), false, false);
    a = __uint_as_float(r[0]); b = __uint_as_float(r[1]);
}
__device__ __forceinline__ void reduce16(const v2f (&s2)[4], const float (&mx)[8], v2f& rs, float& m0, float& m1) {
    float s[8], m[8];
#pragma unroll
    for (int j = 0; j < 4; j++) { s[2*j] = s2[j][0]; s[2*j+1] = s2[j][1]; m[2*j] = mx[2*j]; m[2*j+1] = mx[2*j+1]; }
#pragma unroll
    for (int j = 0; j < 4; j++) { swap32(s[j], s[j+4]); swap32(m[j], m[j+4]); }
    v2f t0 = (v2f){s[0], s[1]} + (v2f){s[4], s[5]}, t1 = (v2f){s[2], s[3]} + (v2f){s[6], s[7]};
#pragma unroll
    for (int j = 0; j < 4; j++) m[j] = vmax(m[j], m[j+4]);
    float a0 = t0[0], a1 = t0[1], b0 = t1[0], b1 = t1[1];
    swap16(a0, b0); swap16(a1, b1); swap16(m[0], m[2]); swap16(m[1], m[3]);
    rs = (v2f){a0, a1} + (v2f){b0, b1};
    m0 = vmax(m[0], m[2]); m1 = vmax(m[1], m[3]);
}
__device__ __forceinline__ void glds4(const void* g, unsigned lds_dst) {
    unsigned keep;
    asm volatile("s_mov_b32 %0, m0\n\ts_mov_b32 m0, %2\n\ts_nop 0\n\tglobal_load_lds_dword %1, off\n\ts_mov_b32 m0, %0" : "=&s"(keep) : "v"(g), "s"(lds_dst) : "memory");
}
__device__ __forceinline__ void blds16(__amdgpu_buffer_rsrc_t r, unsigned voff, unsigned soff, unsigned lds_dst) {
    unsigned keep;
    asm volatile("s_mov_b32 %0, m0\n\ts_mov_b32 m0, %4\n\ts_nop 0\n\tbuffer_load_dwordx4 %1, %2, %3 offen lds\n\ts_mov_b32 m0, %0" : "=&s"(keep) : "v"(voff), "s"(r), "s"(soff), "s"(lds_dst) : "memory");
}
__device__ __forceinline__ unsigned arrive(unsigned waddr, v4f val, unsigned caddr, unsigned raddr, v4f& p0, v4f& p1, v4f& p2, v4f& p3) {
    unsigned old; const unsigned one = 1;
    asm volatile("ds_write_b128 %5, %6\n\ts_mov_b64 exec, 1\n\tds_add_rtn_u32 %0, %7, %8\n\ts_mov_b64 exec, -1\n\t"
                     "ds_read_b128 %1, %9\n\tds_read_b128 %2, %9 offset:3072\n\tds_read_b128 %3, %9 offset:6144\n\tds_read_b128 %4, %9 offset:9216\n\ts_waitcnt lgkmcnt(0)"
                     : "=&v"(old), "=&v"(p0), "=&v"(p1), "=&v"(p2), "=&v"(p3) : "v"(waddr), "v"(val), "v"(caddr), "v"(one), "v"(raddr) : "memory");
    return __builtin_amdgcn_readfirstlane(old);
}
__device__ __forceinline__ unsigned arrive_r(bool wr, unsigned waddr, float val, unsigned caddr, unsigned raddr, v4f& p0, v4f& p1, v4f& p2, v4f& p3) {
    unsigned old; const unsigned one = 1;
    if (wr) asm volatile("ds_write_b32 %0, %1" :: "v"(waddr), "v"(val) : "memory");
    asm volatile("s_mov_b64 exec, 1\n\tds_add_rtn_u32 %0, %5, %6\n\ts_mov_b64 exec, -1\n\t"
                 "ds_read_b128 %1, %7\n\tds_read_b128 %2, %7 offset:16\n\tds_read_b128 %3, %7 offset:32\n\tds_read_b128 %4, %7 offset:48\n\ts_waitcnt lgkmcnt(0)"
                 : "=&v"(old), "=&v"(p0), "=&v"(p1), "=&v"(p2), "=&v"(p3) : "v"(caddr), "v"(one), "v"(raddr) : "memory");
    return __builtin_amdgcn_readfirstlane(old);
}
__device__ __forceinline__ void lds_barrier() {
    asm volatile("s_waitcnt lgkmcnt(0)\n\ts_barrier" ::: "memory");
}
__device__ __forceinline__ v2u tr_read(const char* p) {
    return __builtin_bit_cast(v2u, __builtin_amdgcn_ds_read_tr16_b64_v4i16((LAS v4s*)p));
}
__device__ __forceinline__ v4f smf(v8bf a, v8u b, v4f d, int idx) {
    return __builtin_amdgcn_smfmac_f32_16x16x64_bf16(a, __builtin_bit_cast(v16bf, b), d, idx, 0, 0);
}
__device__ __forceinline__ v8u cat(v4u a, v4u b) { return (v8u){a[0], a[1], a[2], a[3], b[0], b[1], b[2], b[3]}; }
__device__ __forceinline__ unsigned dpos(unsigned d) { return (d & ~7u) | ((d & 1u) << 2) | ((d >> 1) & 3u); }
__device__ __forceinline__ v4u frag(const char* p, int second) {
    const v2u a = tr_read(p), b = tr_read(p + second);
    return (v4u){a[0], a[1], b[0], b[1]};
}

__global__ void __launch_bounds__(NT, 3)
cbam_kernel(const void* __restrict__ x, const void* __restrict__ w1, const void* __restrict__ w2,
            void* __restrict__ out, void* ws) {
    extern __shared__ __attribute__((aligned(16))) char lds[];
    const int t = threadIdx.x, l = t & 63, b = blockIdx.x;
    const int w = __builtin_amdgcn_readfirstlane(t >> 6);
    const int q = l >> 4, m = l & 15;
    const int s3 = w % 3, rg = w / 3;
    unsigned xbase = ((unsigned)b * 16 + rg * 4 + q) * 768 + s3 * 256 + m * 16;
    constexpr unsigned XSTEP = 256 * 16 * 768;
    {
        u64 pc; asm volatile("s_getpc_b64 %0" : "=s"(pc));
        const unsigned line = min(w * 7 + min(l, 6), 75);
        glds4((const char*)(pc & ~(u64)255) + line * 128, L_DUMMY);
        asm volatile("" : "+v"(xbase));
        __builtin_amdgcn_sched_barrier(0);
    }
    const __amdgpu_buffer_rsrc_t rx = __builtin_amdgcn_make_buffer_rsrc((void*)x, 0, XBYTES, 0x00020000);
    const __amdgpu_buffer_rsrc_t ro = __builtin_amdgcn_make_buffer_rsrc(out, 0, XBYTES, 0x00020000);
    const __amdgpu_buffer_rsrc_t rws = __builtin_amdgcn_make_buffer_rsrc(ws, 0, WS_END, 0x00020000);
    const __amdgpu_buffer_rsrc_t rw1 = __builtin_amdgcn_make_buffer_rsrc((void*)w1, 0, C * R * 2, 0x00020000);
    const __amdgpu_buffer_rsrc_t rw2 = __builtin_amdgcn_make_buffer_rsrc((void*)w2, 0, C * R * 2, 0x00020000);

    v4u v[NL];
    asm volatile("" :: "s"(w1), "s"(w2), "s"(out), "s"(ws));
    v[0] = __builtin_amdgcn_raw_buffer_load_b128(rx, xbase, 0, LAUX);
    __builtin_amdgcn_sched_barrier(0);
    if (t < 4) *(unsigned*)(lds + L_CNT + t * 4) = 0;
    lds_barrier();
    unsigned e_old;
    asm volatile("s_load_dword %0, %1, 0x0" : "=s"(e_old) : "s"(ws) : "memory");
    if (w == 8) {
        const unsigned sl = (b >> 3) & 31;
        glds4((const char*)(sl < 16 ? w1 : w2) + ((sl & 15) * 36 + (l < 36 ? l : 35)) * 128, L_DUMMY);
    }
#pragma unroll
    for (int i = 1; i < NL; i++) { v[i] = __builtin_amdgcn_raw_buffer_load_b128(rx, xbase, i * XSTEP, LAUX); __builtin_amdgcn_sched_barrier(0); }

    unsigned wo1[3], wo2[3];
#pragma unroll
    for (int k = 0; k < 3; k++) {
        const unsigned p = 64 * k + l, r1 = p / 12, r2 = p / 48;
        wo1[k] = r1 * 192 + (((p % 12) ^ (((r1 >> 3) & 1) << 1)) << 4);
        wo2[k] = r2 * 768 + (((p % 48) ^ ((r2 | ((w & 1) << 2)) << 1)) << 4);
        asm volatile("" : "+v"(wo1[k]), "+v"(wo2[k]));
    }
    __builtin_amdgcn_sched_barrier(0);

    v2f sm2[4]; float mx[8];
#pragma unroll
    for (int j = 0; j < 4; j++) { mx[2*j] = f_lo(v[0][j]); mx[2*j+1] = f_hi(v[0][j]); sm2[j] = (v2f){mx[2*j], mx[2*j+1]}; }
    __builtin_amdgcn_sched_barrier(0);
#pragma unroll
    for (int i = 1; i < NL - 1; i += 2) {
#pragma unroll
        for (int j = 0; j < 4; j++) {
            const v2f a = {f_lo(v[i][j]), f_hi(v[i][j])}, c = {f_lo(v[i+1][j]), f_hi(v[i+1][j])};
            sm2[j] += a; sm2[j] += c;
            mx[2*j] = vmax3(mx[2*j], a[0], c[0]); mx[2*j+1] = vmax3(mx[2*j+1], a[1], c[1]);
        }
        __builtin_amdgcn_sched_barrier(0);
    }
#pragma unroll
    for (int i = 0; i < NL - 1; i++) asm volatile("" : "+v"(v[i]));
    v2f rs; float rm0, rm1;
    asm volatile("s_waitcnt lgkmcnt(0)" : "+s"(e_old) :: "memory");
    const unsigned nonce = e_old * 0x9E3779B1u + 0x85EBCA6Bu;
    const unsigned gi = m * 4 + q;
    unsigned prd = L_PART + (s3 * 64 + gi) * 16, pwr = prd + rg * 3072, pcn = L_CNT + s3 * 4;
    unsigned ppub = WS_P + (s3 * 8 + (gi >> 3)) * (NWG * 128) + b * 128 + (gi & 7) * 16;
    asm volatile("" : "+v"(prd), "+v"(pwr), "+v"(pcn), "+v"(ppub));
    __builtin_amdgcn_sched_barrier(0);
    {
#pragma unroll
        for (int j = 0; j < 4; j++) { const v2f a = {f_lo(v[NL-1][j]), f_hi(v[NL-1][j])}; sm2[j] += a; mx[2*j] = vmax(mx[2*j], a[0]); mx[2*j+1] = vmax(mx[2*j+1], a[1]); }
        reduce16(sm2, mx, rs, rm0, rm1);
    }
    asm volatile("" : "+v"(v[NL - 1]));
    {
        v4f p0, p1, p2, p3;
        const bool last = arrive(pwr, (v4f){rs[0], rs[1], rm0, rm1}, pcn, prd, p0, p1, p2, p3) == 3;
        const v2f ss = ((v2f){p0[0], p0[1]} + (v2f){p1[0], p1[1]}) + ((v2f){p2[0], p2[1]} + (v2f){p3[0], p3[1]});
        const float m0 = vmax(vmax3(p0[2], p1[2], p2[2]), p3[2]), m1 = vmax(vmax3(p0[3], p1[3], p2[3]), p3[3]);
        v4u g;
        g[0] = __float_as_uint(ss[0]); g[1] = (__float_as_uint(m0) >> 16) | (nonce << 16);
        g[2] = __float_as_uint(ss[1]); g[3] = (__float_as_uint(m1) >> 16) | (nonce & 0xffff0000u);
        asm volatile("" : "+v"(g));
        if (last) __builtin_amdgcn_raw_buffer_store_b128(g, rws, ppub, 0, HAUX);
    }
    lds_barrier();
    if (b < NRED) {
        if (w >= 4) {
            const unsigned off = WS_P + b * (NWG * 128) + (t - 256) * 16;
            const unsigned tag = nonce;
            v4u g[4];
            unsigned miss = 15u;
            for (unsigned spins = 0; miss && spins < SPIN_CAP; spins++) {
                asm volatile("" ::: "memory");
#pragma unroll
                for (int i = 0; i < 4; i++) if (miss >> i & 1) g[i] = __builtin_amdgcn_raw_buffer_load_b128(rws, off, i * 8192, HAUX);
#pragma unroll
                for (int i = 0; i < 4; i++) if (__all(__builtin_amdgcn_perm(g[i][3], g[i][1], 0x07060302u) == tag)) miss &= ~(1u << i);
            }
            float s0 = (__uint_as_float(g[0][0]) + __uint_as_float(g[1][0])) + (__uint_as_float(g[2][0]) + __uint_as_float(g[3][0]));
            float s1 = (__uint_as_float(g[0][2]) + __uint_as_float(g[1][2])) + (__uint_as_float(g[2][2]) + __uint_as_float(g[3][2]));
            float m0 = vmax(vmax3(f_lo(g[0][1]), f_lo(g[1][1]), f_lo(g[2][1])), f_lo(g[3][1]));
            float m1 = vmax(vmax3(f_lo(g[0][3]), f_lo(g[1][3]), f_lo(g[2][3])), f_lo(g[3][3]));
            swap32(s0, s1); s0 += s1;
            swap32(m0, m1); m0 = vmax(m0, m1);
            swap16(s0, m0);
            float r = (q & 1) ? vmax(s0, m0) : s0 + m0;
            const float o = dpp<0x128>(r);
            r = (q & 1) ? vmax(r, o) : r + o;
            v4f a0, a1, b0, b1;
            const bool last = arrive_r((l & 8) == 0, L_RED + (((l & 7) * 4 + q) * 8 + (w - 4)) * 4, r, L_CNT + 12,
                                       L_RED + ((m >> 1) * 4 + (m & 1) * 2) * 32, a0, a1, b0, b1) == 7;
            const v2f s2 = ((v2f){a0[0], a0[1]} + (v2f){a0[2], a0[3]}) + ((v2f){a1[0], a1[1]} + (v2f){a1[2], a1[3]});
            const float s = s2[0] + s2[1];
            const float mm = vmax(vmax3(vmax3(b0[0], b0[1], b0[2]), b0[3], b1[0]), vmax3(b1[1], b1[2], b1[3]));
            v2u gg;
            gg[0] = (pk_bf16(s * (1.0f / HW), 0.f) & 0xffffu) | (__float_as_uint(mm) & 0xffff0000u);
            gg[1] = nonce;
            unsigned fo = WS_F + q * 3072 + (b * 16 + m) * 8;
            asm volatile("" : "+v"(gg), "+v"(fo));
            if (last) {
#pragma unroll
                for (int rr = 0; rr < NREP; rr += 4) __builtin_amdgcn_raw_buffer_store_b64(gg, rws, fo + rr * 3072, 0, HAUX);
            }
        }
        lds_barrier();
    }
    {
#pragma unroll
        for (int i = 0; i < 6; i++) blds16(rw1, wo1[i % 3], w * 6144 + (i / 3) * 3072, L_W1 + w * 6144 + i * 1024);
#pragma unroll
        for (int i = 0; i < 6; i++) blds16(rw2, wo2[i % 3], w * 6144 + (i / 3) * 3072, L_W2 + w * 6144 + i * 1024);
        asm volatile("s_waitcnt vmcnt(0)" ::: "memory");
    }
    lds_barrier();
    v8u fb[8];
    {
        const int qq = (l & 15) >> 2, pq = l & 3;
        const v4u zero = {0u, 0u, 0u, 0u};
        if (w < 6) {
            const char* p1 = lds + L_W1 + (8 * q + qq) * 192 + ((2 * w + (pq >> 1)) ^ ((q & 1) << 1)) * 16 + (pq & 1) * 8;
#pragma unroll
            for (int s = 0; s < 6; s++) fb[s] = cat(frag(p1 + 2 * s * (32 * 192), 4 * 192), frag(p1 + (2 * s + 1) * (32 * 192), 4 * 192));
#pragma unroll
            for (int s = 0; s < 6; s++) asm volatile("" : "+v"(fb[s]));
        } else {
            const char* p2 = lds + L_W2 + (8 * q + qq) * 768 + (pq & 1) * 8;
            const int g2 = (qq | ((q & 1) << 2)) << 1;
#pragma unroll
            for (int n = 0; n < 4; n++) {
                const char* pn = p2 + ((8 * (w - 6) + 2 * n + (pq >> 1)) ^ g2) * 16;
                fb[n] = cat(frag(pn, 4 * 768), frag(pn + 32 * 768, 4 * 768));
                fb[4 + n] = cat(frag(pn + 64 * 768, 4 * 768), zero);
            }
#pragma unroll
            for (int s = 0; s < 8; s++) asm volatile("" : "+v"(fb[s]));
        }
    }
    unsigned oa1 = L_IN + ((l >> 1) & 1) * 768 + (8 * q + 4 * (l & 1)) * 4, oa2 = L_H + ((l >> 1) & 1) * 256 + (8 * q + 4 * (l & 1)) * 4;
    unsigned ohw = L_H + (q & 1) * 256 + dpos(w * 8 + (m >> 1)) * 4 + (m & 1) * 2;
    unsigned osw = L_SC + ((w - 6) * 64 + q * 16 + m) * 4, osr = L_SC + (s3 * 128 + m * 8) * 4;
    int sidx = (l & 1) ? 0xEEEE : 0x4444;
    asm volatile("" : "+v"(oa1), "+v"(oa2), "+v"(ohw), "+v"(osw), "+v"(osr), "+v"(sidx));
    if (w == 6 && l < 32) *(unsigned*)(lds + L_H + (l >> 4) * 256 + 192 + (l & 15) * 4) = 0;
    if (w >= 9) {
        const unsigned u = t - 576;
        const unsigned off = WS_F + (b & (NREP - 1)) * 3072 + u * 16;
        v4u g;
        for (unsigned spins = 0;; spins++) {
            asm volatile("" ::: "memory");
            g = __builtin_amdgcn_raw_buffer_load_b128(rws, off, 0, HAUX);
            const bool ok = (g[1] == nonce) & (g[3] == nonce);
            if (__all(ok) || spins > SPIN_CAP) break;
        }
        *(unsigned*)(lds + L_IN + dpos(u) * 4) = __builtin_amdgcn_perm(g[2], g[0], 0x05040100u);
        *(unsigned*)(lds + L_IN + 768 + dpos(u) * 4) = __builtin_amdgcn_perm(g[2], g[0], 0x07060302u);
    }
    lds_barrier();
    if (w < 6) {
        v4f d = {0.f, 0.f, 0.f, 0.f};
        v8bf a[6];
#pragma unroll
        for (int s = 0; s < 6; s++) a[s] = *(const v8bf*)(lds + oa1 + s * 128);
        __builtin_amdgcn_sched_barrier(0);
#pragma unroll
        for (int s = 0; s < 6; s++) d = smf(a[s], fb[s], d, sidx);
        float h = rbf((q & 1) ? d[2] + d[3] : d[0] + d[1]);
        h = h * (h > 0.f ? 1.f : 0.f);
        if (q < 2) *(unsigned short*)(lds + ohw) = (unsigned short)(__float_as_uint(h) >> 16);
    } else if (w == 6 && b == 0 && l == 0) {
        __builtin_amdgcn_raw_buffer_store_b32(nonce, rws, WS_E, 0, HAUX);
    }
    lds_barrier();
    if (w >= 6) {
        v4f d[4];
        const v8bf a0 = *(const v8bf*)(lds + oa2), a1 = *(const v8bf*)(lds + oa2 + 128);
        __builtin_amdgcn_sched_barrier(0);
#pragma unroll
        for (int n = 0; n < 4; n++) d[n] = smf(a0, fb[n], (v4f){0.f, 0.f, 0.f, 0.f}, sidx);
#pragma unroll
        for (int n = 0; n < 4; n++) d[n] = smf(a1, fb[4 + n], d[n], sidx);
        const v4f dl = (q & 1) ? d[1] : d[0], dh = (q & 1) ? d[3] : d[2], dq = (q & 2) ? dh : dl;
        const unsigned pz = pk_bf16(dq[0] + dq[1], dq[2] + dq[3]);
        const float z = rbf(f_lo(pz) + f_hi(pz));
        const float sc = rbf(__builtin_amdgcn_rcpf(1.0f + __expf(-z)));
        *(float*)(lds + osw) = sc;
    }
    lds_barrier();
    {
        const v4f sa = *(const v4f*)(lds + osr), sb = *(const v4f*)(lds + osr + 16);
        const v2f sc[4] = {{sa[0], sa[1]}, {sa[2], sa[3]}, {sb[0], sb[1]}, {sb[2], sb[3]}};
        const unsigned pace = b < NRED ? PACE_RED : PACE;
        unsigned tnext = (unsigned)__builtin_amdgcn_s_memrealtime();
#pragma unroll
        for (int i = 0; i < NL; i++) {
            v4u o;
#pragma unroll
            for (int j = 0; j < 4; j++) { const v2f r = (v2f){f_lo(v[i][j]), f_hi(v[i][j])} * sc[j]; o[j] = pk_bf16(r[0], r[1]); }
            if (i > 0) { tnext += pace; while ((int)((unsigned)__builtin_amdgcn_s_memrealtime() - tnext) < 0) __builtin_amdgcn_s_sleep(1); }
            __builtin_amdgcn_raw_buffer_store_b128(o, ro, xbase, i * XSTEP, SAUX);
            __builtin_amdgcn_sched_barrier(0);
            asm volatile("s_nop 1");
            __builtin_amdgcn_sched_barrier(0);
        }
    }
}

extern "C" void kernel_launch(void* const* d_in, const int* in_sizes, int n_in,
                              void* d_out, int out_size, void* d_ws, size_t ws_size,
                              hipStream_t stream) {
    (void)in_sizes; (void)n_in; (void)out_size; (void)ws_size;
    (void)hipFuncSetAttribute((const void*)cbam_kernel, hipFuncAttributeMaxDynamicSharedMemorySize, LDS_BYTES);
    cbam_kernel<<<NWG, NT, LDS_BYTES, stream>>>(d_in[0], d_in[1], d_in[2], d_out, d_ws);
}
```

```cpp
#include <hip/hip_runtime.h>
#include <stdint.h>

constexpr int LAUX = 19;
constexpr int SAUX = 18;
constexpr int HAUX = 16;
constexpr unsigned PACE = 40;
constexpr unsigned PACE_RED = 20;
constexpr int NREP = 4;

typedef unsigned v4u __attribute__((ext_vector_type(4)));
typedef unsigned v2u __attribute__((ext_vector_type(2)));
typedef float v4f __attribute__((ext_vector_type(4)));
typedef float v2f __attribute__((ext_vector_type(2)));
typedef short v4s __attribute__((ext_vector_type(4)));
typedef __bf16 v2bf __attribute__((ext_vector_type(2)));
typedef __bf16 v8bf __attribute__((ext_vector_type(8)));
typedef __bf16 v16bf __attribute__((ext_vector_type(16)));
typedef unsigned v8u __attribute__((ext_vector_type(8)));
typedef unsigned long long u64;
#define LAS __attribute__((address_space(3)))

constexpr int HW = 65536, C = 384, R = 96;
constexpr int NWG = 256, NT = 768, NL = 16;
constexpr unsigned XBYTES = (unsigned)HW * C * 2;
constexpr int NRED = 24;
constexpr unsigned WS_E = 0;
constexpr unsigned WS_P = 4096;
constexpr unsigned WS_F = WS_P + NRED * NWG * 128;
constexpr unsigned WS_END = WS_F + NREP * 3072;
constexpr unsigned L_W1 = 0;
constexpr unsigned L_W2 = L_W1 + C * R * 2;
constexpr unsigned L_PART = L_W2 + C * R * 2;
constexpr unsigned L_IN = L_PART;
constexpr unsigned L_H = L_IN + 1536;
constexpr unsigned L_SC = L_H + 512;
constexpr unsigned L_RED = L_PART + 4 * 192 * 16;
constexpr unsigned L_CNT = L_RED + 1024;
constexpr unsigned L_DUMMY = L_CNT + 16;
constexpr unsigned L_END = L_DUMMY + 256;
constexpr unsigned LDS_BYTES = L_END;
typedef char lds_layout_check[(LDS_BYTES <= 160 * 1024 && LDS_BYTES > 80 * 1024 && L_SC + 1536 <= L_RED) ? 1 : -1];
constexpr unsigned SPIN_CAP = 1u << 20;

__device__ __forceinline__ float f_lo(unsigned d) { return __uint_as_float(d << 16); }
__device__ __forceinline__ float f_hi(unsigned d) { return __uint_as_float(d & 0xffff0000u); }
__device__ __forceinline__ float vmax(float a, float b) { return __builtin_elementwise_maximum(a, b); }
__device__ __forceinline__ float vmax3(float a, float b, float c) { return vmax(vmax(a, b), c); }
__device__ __forceinline__ unsigned pk_bf16(float a, float b) { v2bf r = {(__bf16)a, (__bf16)b}; return __builtin_bit_cast(unsigned, r); }
__device__ __forceinline__ float rbf(float a) { return __uint_as_float(pk_bf16(a, a) << 16); }
template <int CTRL> __device__ __forceinline__ float dpp(float x) {
    return __builtin_bit_cast(float, __builtin_amdgcn_mov_dpp(__builtin_bit_cast(int, x), CTRL, 0xf, 0xf, true));
}
__device__ __forceinline__ void swap32(float& a, float& b) {
    auto r = __builtin_amdgcn_permlane32_swap(__float_as_uint(a), __float_as_uint(b), false, false);
    a = __uint_as_float(r[0]); b = __uint_as_float(r[1]);
}
__device__ __forceinline__ void swap16(float& a, float& b) {
    auto r = __builtin_amdgcn_permlane16_swap(__float_as_uint(a), __float_as_uint(b), false, false);
    a = __uint_as_float(r[0]); b = __uint_as_float(r[1]);
}
__device__ __forceinline__ void reduce16(const v2f (&s2)[4], const float (&mx)[8], v2f& rs, float& m0, float& m1) {
    float s[8], m[8];
#pragma unroll
    for (int j = 0; j < 4; j++) { s[2*j] = s2[j][0]; s[2*j+1] = s2[j][1]; m[2*j] = mx[2*j]; m[2*j+1] = mx[2*j+1]; }
#pragma unroll
    for (int j = 0; j < 4; j++) { swap32(s[j], s[j+4]); swap32(m[j], m[j+4]); }
    v2f t0 = (v2f){s[0], s[1]} + (v2f){s[4], s[5]}, t1 = (v2f){s[2], s[3]} + (v2f){s[6], s[7]};
#pragma unroll
    for (int j = 0; j < 4; j++) m[j] = vmax(m[j], m[j+4]);
    float a0 = t0[0], a1 = t0[1], b0 = t1[0], b1 = t1[1];
    swap16(a0, b0); swap16(a1, b1); swap16(m[0], m[2]); swap16(m[1], m[3]);
    rs = (v2f){a0, a1} + (v2f){b0, b1};
    m0 = vmax(m[0], m[2]); m1 = vmax(m[1], m[3]);
}
__device__ __forceinline__ void glds4(const void* g, unsigned lds_dst) {
    unsigned keep;
    asm volatile("s_mov_b32 %0, m0\n\ts_mov_b32 m0, %2\n\ts_nop 0\n\tglobal_load_lds_dword %1, off\n\ts_mov_b32 m0, %0" : "=&s"(keep) : "v"(g), "s"(lds_dst) : "memory");
}
__device__ __forceinline__ void blds16(__amdgpu_buffer_rsrc_t r, unsigned voff, unsigned soff, unsigned lds_dst) {
    unsigned keep;
    asm volatile("s_mov_b32 %0, m0\n\ts_mov_b32 m0, %4\n\ts_nop 0\n\tbuffer_load_dwordx4 %1, %2, %3 offen lds\n\ts_mov_b32 m0, %0" : "=&s"(keep) : "v"(voff), "s"(r), "s"(soff), "s"(lds_dst) : "memory");
}
__device__ __forceinline__ unsigned arrive(unsigned waddr, v4f val, unsigned caddr, unsigned raddr, v4f& p0, v4f& p1, v4f& p2, v4f& p3) {
    unsigned old; const unsigned one = 1;
    asm volatile("ds_write_b128 %5, %6\n\ts_mov_b64 exec, 1\n\tds_add_rtn_u32 %0, %7, %8\n\ts_mov_b64 exec, -1\n\t"
                     "ds_read_b128 %1, %9\n\tds_read_b128 %2, %9 offset:3072\n\tds_read_b128 %3, %9 offset:6144\n\tds_read_b128 %4, %9 offset:9216\n\ts_waitcnt lgkmcnt(0)"
                     : "=&v"(old), "=&v"(p0), "=&v"(p1), "=&v"(p2), "=&v"(p3) : "v"(waddr), "v"(val), "v"(caddr), "v"(one), "v"(raddr) : "memory");
    return __builtin_amdgcn_readfirstlane(old);
}
__device__ __forceinline__ unsigned arrive_r(bool wr, unsigned waddr, float val, unsigned caddr, unsigned raddr, v4f& p0, v4f& p1, v4f& p2, v4f& p3) {
    unsigned old; const unsigned one = 1;
    if (wr) asm volatile("ds_write_b32 %0, %1" :: "v"(waddr), "v"(val) : "memory");
    asm volatile("s_mov_b64 exec, 1\n\tds_add_rtn_u32 %0, %5, %6\n\ts_mov_b64 exec, -1\n\t"
                 "ds_read_b128 %1, %7\n\tds_read_b128 %2, %7 offset:16\n\tds_read_b128 %3, %7 offset:32\n\tds_read_b128 %4, %7 offset:48\n\ts_waitcnt lgkmcnt(0)"
                 : "=&v"(old), "=&v"(p0), "=&v"(p1), "=&v"(p2), "=&v"(p3) : "v"(caddr), "v"(one), "v"(raddr) : "memory");
    return __builtin_amdgcn_readfirstlane(old);
}
__device__ __forceinline__ void lds_barrier() {
    asm volatile("s_waitcnt lgkmcnt(0)\n\ts_barrier" ::: "memory");
}
__device__ __forceinline__ v2u tr_read(const char* p) {
    return __builtin_bit_cast(v2u, __builtin_amdgcn_ds_read_tr16_b64_v4i16((LAS v4s*)p));
}
__device__ __forceinline__ v4f smf(v8bf a, v8u b, v4f d, int idx) {
    return __builtin_amdgcn_smfmac_f32_16x16x64_bf16(a, __builtin_bit_cast(v16bf, b), d, idx, 0, 0);
}
__device__ __forceinline__ v8u cat(v4u a, v4u b) { return (v8u){a[0], a[1], a[2], a[3], b[0], b[1], b[2], b[3]}; }
__device__ __forceinline__ unsigned dpos(unsigned d) { return (d & ~7u) | ((d & 1u) << 2) | ((d >> 1) & 3u); }
__device__ __forceinline__ v4u frag(const char* p, int second) {
    const v2u a = tr_read(p), b = tr_read(p + second);
    return (v4u){a[0], a[1], b[0], b[1]};
}

__global__ void __launch_bounds__(NT, 3)
cbam_kernel(const void* x_arg, const void* w1_arg, const void* w2_arg, void* out_arg, void* ws_arg) {
    extern __shared__ __attribute__((aligned(16))) char lds[];
    const int t = threadIdx.x;
    v8u ka; v2u kb;
    {
        u64 pc; unsigned off;
        asm volatile("s_load_dwordx8 %2, %4, 0x0\n\ts_load_dwordx2 %3, %4, 0x20\n\t"
                     "s_getpc_b64 %0\n\tv_mul_u32_u24 %1, 0xcd, %5\n\tv_lshrrev_b32 %1, 11, %1\n\tv_lshlrev_b32 %1, 7, %1\n\t"
                     "s_mov_b32 m0, %6\n\ts_nop 0\n\tglobal_load_lds_dword %1, %0"
                     : "=&s"(pc), "=&v"(off), "=&s"(ka), "=&s"(kb) : "s"(__builtin_amdgcn_kernarg_segment_ptr()), "v"(t), "i"(L_DUMMY) : "memory");
        __builtin_amdgcn_sched_barrier(0);
    }
    const int l = t & 63, b = blockIdx.x;
    const int w = __builtin_amdgcn_readfirstlane(t >> 6);
    const int q = l >> 4, m = l & 15;
    const int s3 = w % 3, rg = w / 3;
    unsigned xbase = ((unsigned)b * 16 + rg * 4 + q) * 768 + s3 * 256 + m * 16;
    constexpr unsigned XSTEP = 256 * 16 * 768;
    asm volatile("s_waitcnt lgkmcnt(0)" : "+s"(ka), "+s"(kb), "+v"(xbase) :: "memory");
    __builtin_amdgcn_sched_barrier(0);
    const void* x = (const void*)(((u64)ka[1] << 32) | ka[0]);
    const void* w1 = (const void*)(((u64)ka[3] << 32) | ka[2]);
    const void* w2 = (const void*)(((u64)ka[5] << 32) | ka[4]);
    void* out = (void*)(((u64)ka[7] << 32) | ka[6]);
    void* ws = (void*)(((u64)kb[1] << 32) | kb[0]);
    const __amdgpu_buffer_rsrc_t rx = __builtin_amdgcn_make_buffer_rsrc((void*)x, 0, XBYTES, 0x00020000);
    const __amdgpu_buffer_rsrc_t ro = __builtin_amdgcn_make_buffer_rsrc(out, 0, XBYTES, 0x00020000);
    const __amdgpu_buffer_rsrc_t rws = __builtin_amdgcn_make_buffer_rsrc(ws, 0, WS_END, 0x00020000);
    const __amdgpu_buffer_rsrc_t rw1 = __builtin_amdgcn_make_buffer_rsrc((void*)w1, 0, C * R * 2, 0x00020000);
    const __amdgpu_buffer_rsrc_t rw2 = __builtin_amdgcn_make_buffer_rsrc((void*)w2, 0, C * R * 2, 0x00020000);

    v4u v[NL];
    v[0] = __builtin_amdgcn_raw_buffer_load_b128(rx, xbase, 0, LAUX);
    __builtin_amdgcn_sched_barrier(0);
    if (t < 4) *(unsigned*)(lds + L_CNT + t * 4) = 0;
    lds_barrier();
    unsigned e_old;
    asm volatile("s_load_dword %0, %1, 0x0" : "=s"(e_old) : "s"(ws) : "memory");
    if (w == 8) {
        const unsigned sl = (b >> 3) & 31;
        glds4((const char*)(sl < 16 ? w1 : w2) + ((sl & 15) * 36 + (l < 36 ? l : 35)) * 128, L_DUMMY);
    }
#pragma unroll
    for (int i = 1; i < NL; i++) { v[i] = __builtin_amdgcn_raw_buffer_load_b128(rx, xbase, i * XSTEP, LAUX); __builtin_amdgcn_sched_barrier(0); }

    unsigned wo1[3], wo2[3];
#pragma unroll
    for (int k = 0; k < 3; k++) {
        const unsigned p = 64 * k + l, r1 = p / 12, r2 = p / 48;
        wo1[k] = r1 * 192 + (((p % 12) ^ (((r1 >> 3) & 1) << 1)) << 4);
        wo2[k] = r2 * 768 + (((p % 48) ^ ((r2 | ((w & 1) << 2)) << 1)) << 4);
        asm volatile("" : "+v"(wo1[k]), "+v"(wo2[k]));
    }
    __builtin_amdgcn_sched_barrier(0);

    v2f sm2[4]; float mx[8];
#pragma unroll
    for (int j = 0; j < 4; j++) { mx[2*j] = f_lo(v[0][j]); mx[2*j+1] = f_hi(v[0][j]); sm2[j] = (v2f){mx[2*j], mx[2*j+1]}; }
    __builtin_amdgcn_sched_barrier(0);
#pragma unroll
    for (int i = 1; i < NL - 1; i += 2) {
#pragma unroll
        for (int j = 0; j < 4; j++) {
            const v2f a = {f_lo(v[i][j]), f_hi(v[i][j])}, c = {f_lo(v[i+1][j]), f_hi(v[i+1][j])};
            sm2[j] += a; sm2[j] += c;
            mx[2*j] = vmax3(mx[2*j], a[0], c[0]); mx[2*j+1] = vmax3(mx[2*j+1], a[1], c[1]);
        }
        __builtin_amdgcn_sched_barrier(0);
    }
#pragma unroll
    for (int i = 0; i < NL - 1; i++) asm volatile("" : "+v"(v[i]));
    v2f rs; float rm0, rm1;
    asm volatile("s_waitcnt lgkmcnt(0)" : "+s"(e_old) :: "memory");
    const unsigned nonce = e_old * 0x9E3779B1u + 0x85EBCA6Bu;
    const unsigned gi = m * 4 + q;
    unsigned prd = L_PART + (s3 * 64 + gi) * 16, pwr = prd + rg * 3072, pcn = L_CNT + s3 * 4;
    unsigned ppub = WS_P + (s3 * 8 + (gi >> 3)) * (NWG * 128) + b * 128 + (gi & 7) * 16;
    asm volatile("" : "+v"(prd), "+v"(pwr), "+v"(pcn), "+v"(ppub));
    __builtin_amdgcn_sched_barrier(0);
    {
#pragma unroll
        for (int j = 0; j < 4; j++) { const v2f a = {f_lo(v[NL-1][j]), f_hi(v[NL-1][j])}; sm2[j] += a; mx[2*j] = vmax(mx[2*j], a[0]); mx[2*j+1] = vmax(mx[2*j+1], a[1]); }
        reduce16(sm2, mx, rs, rm0, rm1);
    }
    asm volatile("" : "+v"(v[NL - 1]));
    {
        v4f p0, p1, p2, p3;
        const bool last = arrive(pwr, (v4f){rs[0], rs[1], rm0, rm1}, pcn, prd, p0, p1, p2, p3) == 3;
        const v2f ss = ((v2f){p0[0], p0[1]} + (v2f){p1[0], p1[1]}) + ((v2f){p2[0], p2[1]} + (v2f){p3[0], p3[1]});
        const float m0 = vmax(vmax3(p0[2], p1[2], p2[2]), p3[2]), m1 = vmax(vmax3(p0[3], p1[3], p2[3]), p3[3]);
        v4u g;
        g[0] = __float_as_uint(ss[0]); g[1] = (__float_as_uint(m0) >> 16) | (nonce << 16);
        g[2] = __float_as_uint(ss[1]); g[3] = (__float_as_uint(m1) >> 16) | (nonce & 0xffff0000u);
        asm volatile("" : "+v"(g));
        if (last) __builtin_amdgcn_raw_buffer_store_b128(g, rws, ppub, 0, HAUX);
    }
    lds_barrier();
    if (b < NRED) {
        if (w >= 4) {
            const unsigned off = WS_P + b * (NWG * 128) + (t - 256) * 16;
            const unsigned tag = nonce;
            v4u g[4];
            unsigned miss = 15u;
            for (unsigned spins = 0; miss && spins < SPIN_CAP; spins++) {
                asm volatile("" ::: "memory");
#pragma unroll
                for (int i = 0; i < 4; i++) if (miss >> i & 1) g[i] = __builtin_amdgcn_raw_buffer_load_b128(rws, off, i * 8192, HAUX);
#pragma unroll
                for (int i = 0; i < 4; i++) if (__all(__builtin_amdgcn_perm(g[i][3], g[i][1], 0x07060302u) == tag)) miss &= ~(1u << i);
            }
            float s0 = (__uint_as_float(g[0][0]) + __uint_as_float(g[1][0])) + (__uint_as_float(g[2][0]) + __uint_as_float(g[3][0]));
            float s1 = (__uint_as_float(g[0][2]) + __uint_as_float(g[1][2])) + (__uint_as_float(g[2][2]) + __uint_as_float(g[3][2]));
            float m0 = vmax(vmax3(f_lo(g[0][1]), f_lo(g[1][1]), f_lo(g[2][1])), f_lo(g[3][1]));
            float m1 = vmax(vmax3(f_lo(g[0][3]), f_lo(g[1][3]), f_lo(g[2][3])), f_lo(g[3][3]));
            swap32(s0, s1); s0 += s1;
            swap32(m0, m1); m0 = vmax(m0, m1);
            swap16(s0, m0);
            float r = (q & 1) ? vmax(s0, m0) : s0 + m0;
            const float o = dpp<0x128>(r);
            r = (q & 1) ? vmax(r, o) : r + o;
            v4f a0, a1, b0, b1;
            const bool last = arrive_r((l & 8) == 0, L_RED + (((l & 7) * 4 + q) * 8 + (w - 4)) * 4, r, L_CNT + 12,
                                       L_RED + ((m >> 1) * 4 + (m & 1) * 2) * 32, a0, a1, b0, b1) == 7;
            const v2f s2 = ((v2f){a0[0], a0[1]} + (v2f){a0[2], a0[3]}) + ((v2f){a1[0], a1[1]} + (v2f){a1[2], a1[3]});
            const float s = s2[0] + s2[1];
            const float mm = vmax(vmax3(vmax3(b0[0], b0[1], b0[2]), b0[3], b1[0]), vmax3(b1[1], b1[2], b1[3]));
            v2u gg;
            gg[0] = (pk_bf16(s * (1.0f / HW), 0.f) & 0xffffu) | (__float_as_uint(mm) & 0xffff0000u);
            gg[1] = nonce;
            unsigned fo = WS_F + q * 3072 + (b * 16 + m) * 8;
            asm volatile("" : "+v"(gg), "+v"(fo));
            if (last) {
#pragma unroll
                for (int rr = 0; rr < NREP; rr += 4) __builtin_amdgcn_raw_buffer_store_b64(gg, rws, fo + rr * 3072, 0, HAUX);
            }
        }
        lds_barrier();
    }
    {
#pragma unroll
        for (int i = 0; i < 6; i++) blds16(rw1, wo1[i % 3], w * 6144 + (i / 3) * 3072, L_W1 + w * 6144 + i * 1024);
#pragma unroll
        for (int i = 0; i < 6; i++) blds16(rw2, wo2[i % 3], w * 6144 + (i / 3) * 3072, L_W2 + w * 6144 + i * 1024);
        asm volatile("s_waitcnt vmcnt(0)" ::: "memory");
    }
    lds_barrier();
    v8u fb[8];
    {
        const int qq = (l & 15) >> 2, pq = l & 3;
        const v4u zero = {0u, 0u, 0u, 0u};
        if (w < 6) {
            const char* p1 = lds + L_W1 + (8 * q + qq) * 192 + ((2 * w + (pq >> 1)) ^ ((q & 1) << 1)) * 16 + (pq & 1) * 8;
#pragma unroll
            for (int s = 0; s < 6; s++) fb[s] = cat(frag(p1 + 2 * s * (32 * 192), 4 * 192), frag(p1 + (2 * s + 1) * (32 * 192), 4 * 192));
#pragma unroll
            for (int s = 0; s < 6; s++) asm volatile("" : "+v"(fb[s]));
        } else {
            const char* p2 = lds + L_W2 + (8 * q + qq) * 768 + (pq & 1) * 8;
            const int g2 = (qq | ((q & 1) << 2)) << 1;
#pragma unroll
            for (int n = 0; n < 4; n++) {
                const char* pn = p2 + ((8 * (w - 6) + 2 * n + (pq >> 1)) ^ g2) * 16;
                fb[n] = cat(frag(pn, 4 * 768), frag(pn + 32 * 768, 4 * 768));
                fb[4 + n] = cat(frag(pn + 64 * 768, 4 * 768), zero);
            }
#pragma unroll
            for (int s = 0; s < 8; s++) asm volatile("" : "+v"(fb[s]));
        }
    }
    unsigned oa1 = L_IN + ((l >> 1) & 1) * 768 + (8 * q + 4 * (l & 1)) * 4, oa2 = L_H + ((l >> 1) & 1) * 256 + (8 * q + 4 * (l & 1)) * 4;
    unsigned ohw = L_H + (q & 1) * 256 + dpos(w * 8 + (m >> 1)) * 4 + (m & 1) * 2;
    unsigned osw = L_SC + ((w - 6) * 64 + q * 16 + m) * 4, osr = L_SC + (s3 * 128 + m * 8) * 4;
    int sidx = (l & 1) ? 0xEEEE : 0x4444;
    asm volatile("" : "+v"(oa1), "+v"(oa2), "+v"(ohw), "+v"(osw), "+v"(osr), "+v"(sidx));
    if (w == 6 && l < 32) *(unsigned*)(lds + L_H + (l >> 4) * 256 + 192 + (l & 15) * 4) = 0;
    if (w >= 9) {
        const unsigned u = t - 576;
        const unsigned off = WS_F + (b & (NREP - 1)) * 3072 + u * 16;
        v4u g;
        for (unsigned spins = 0;; spins++) {
            asm volatile("" ::: "memory");
            g = __builtin_amdgcn_raw_buffer_load_b128(rws, off, 0, HAUX);
            const bool ok = (g[1] == nonce) & (g[3] == nonce);
            if (__all(ok) || spins > SPIN_CAP) break;
        }
        *(unsigned*)(lds + L_IN + dpos(u) * 4) = __builtin_amdgcn_perm(g[2], g[0], 0x05040100u);
        *(unsigned*)(lds + L_IN + 768 + dpos(u) * 4) = __builtin_amdgcn_perm(g[2], g[0], 0x07060302u);
    }
    lds_barrier();
    if (w < 6) {
        v4f d = {0.f, 0.f, 0.f, 0.f};
        v8bf a[6];
#pragma unroll
        for (int s = 0; s < 6; s++) a[s] = *(const v8bf*)(lds + oa1 + s * 128);
        __builtin_amdgcn_sched_barrier(0);
#pragma unroll
        for (int s = 0; s < 6; s++) d = smf(a[s], fb[s], d, sidx);
        float h = rbf((q & 1) ? d[2] + d[3] : d[0] + d[1]);
        h = h * (h > 0.f ? 1.f : 0.f);
        if (q < 2) *(unsigned short*)(lds + ohw) = (unsigned short)(__float_as_uint(h) >> 16);
    } else if (w == 6 && b == 0 && l == 0) {
        __builtin_amdgcn_raw_buffer_store_b32(nonce, rws, WS_E, 0, HAUX);
    }
    lds_barrier();
    if (w >= 6) {
        v4f d[4];
        const v8bf a0 = *(const v8bf*)(lds + oa2), a1 = *(const v8bf*)(lds + oa2 + 128);
        __builtin_amdgcn_sched_barrier(0);
#pragma unroll
        for (int n = 0; n < 4; n++) d[n] = smf(a0, fb[n], (v4f){0.f, 0.f, 0.f, 0.f}, sidx);
#pragma unroll
        for (int n = 0; n < 4; n++) d[n] = smf(a1, fb[4 + n], d[n], sidx);
        const v4f dl = (q & 1) ? d[1] : d[0], dh = (q & 1) ? d[3] : d[2], dq = (q & 2) ? dh : dl;
        const unsigned pz = pk_bf16(dq[0] + dq[1], dq[2] + dq[3]);
        const float z = rbf(f_lo(pz) + f_hi(pz));
        const float sc = rbf(__builtin_amdgcn_rcpf(1.0f + __expf(-z)));
        *(float*)(lds + osw) = sc;
    }
    lds_barrier();
    {
        const v4f sa = *(const v4f*)(lds + osr), sb = *(const v4f*)(lds + osr + 16);
        const v2f sc[4] = {{sa[0], sa[1]}, {sa[2], sa[3]}, {sb[0], sb[1]}, {sb[2], sb[3]}};
        const unsigned pace = b < NRED ? PACE_RED : PACE;
        unsigned tnext = (unsigned)__builtin_amdgcn_s_memrealtime();
#pragma unroll
        for (int i = 0; i < NL; i++) {
            v4u o;
#pragma unroll
            for (int j = 0; j < 4; j++) { const v2f r = (v2f){f_lo(v[i][j]), f_hi(v[i][j])} * sc[j]; o[j] = pk_bf16(r[0], r[1]); }
            if (i > 0) { tnext += pace; while ((int)((unsigned)__builtin_amdgcn_s_memrealtime() - tnext) < 0) __builtin_amdgcn_s_sleep(1); }
            __builtin_amdgcn_raw_buffer_store_b128(o, ro, xbase, i * XSTEP, SAUX);
            __builtin_amdgcn_sched_barrier(0);
            asm volatile("s_nop 1");
            __builtin_amdgcn_sched_barrier(0);
        }
    }
}

extern "C" void kernel_launch(void* const* d_in, const int* in_sizes, int n_in,
                              void* d_out, int out_size, void* d_ws, size_t ws_size,
                              hipStream_t stream) {
    (void)in_sizes; (void)n_in; (void)out_size; (void)ws_size;
    (void)hipFuncSetAttribute((const void*)cbam_kernel, hipFuncAttributeMaxDynamicSharedMemorySize, LDS_BYTES);
    cbam_kernel<<<NWG, NT, LDS_BYTES, stream>>>(d_in[0], d_in[1], d_in[2], d_out, d_ws);
}
```
